```python
import torch

def _softmax(x, axis=-1):
    return torch.softmax(x, dim=axis)
def _gelu(x):
    return torch.nn.functional.gelu(x, approximate="tanh")
def _silu(x):
    return torch.nn.functional.silu(x)
def _sigmoid(x):
    return torch.sigmoid(x)
def _layernorm(x, axis=-1, eps=1e-5):
    m = x.mean(dim=axis, keepdim=True)
    v = x.var(dim=axis, keepdim=True, unbiased=False)
    return (x - m) / torch.sqrt(v + eps)
def _scatter_add(tgt, idx, src, axis=0):
    return tgt.clone().index_add_(axis, idx, src)
def _segment_sum(src, seg, n):
    out = torch.zeros((n,) + src.shape[1:], dtype=src.dtype)
    return out.index_add_(0, seg, src)
def _pad(x, pw):
    flat = tuple(v for pair in reversed(pw) for v in pair)
    return torch.nn.functional.pad(x, flat)

kernel_name = 'gated_mix__gqa_block_cached__glu_gated_swiglu__decode'

def setup_inputs():

    _w = (lambda a: (lambda t: t.to(torch.bfloat16) if t.is_floating_point() else t)(torch.as_tensor(a)))
    def _A_setup():
        B, Sq, D = 2, 1, 448
        Hq, Hkv, Sk, Dh = 16, 8, 32768, 256
        assert Hq % Hkv == 0, (Hq, Hkv)
        return {
            "x": _w((lambda *s: torch.randn(*s, dtype=torch.bfloat16) * 0.05)(B, Sq, D)),
            "k_cache": _w((lambda *s: torch.randn(*s, dtype=torch.bfloat16) * 3.2)(B, Hkv, Sk, Dh)),
            "v_cache": _w((lambda *s: torch.randn(*s, dtype=torch.bfloat16) * 0.05)(B, Hkv, Sk, Dh)),
            "wq": _w((lambda *s: torch.randn(*s, dtype=torch.bfloat16) * 3.2)(D, Hq * Dh) * (D ** -0.5)),
            "wo": _w((lambda *s: torch.randn(*s, dtype=torch.bfloat16) * 0.05)(Hq * Dh, D) * ((Hq * Dh) ** -0.5)),
        }
    _A_in = _A_setup()

    def _B_setup():
        B, S, D, F = 2, 1, 448, 1792
        return {
            "x": _w((lambda *s: torch.randn(*s, dtype=torch.bfloat16) * 0.05)(B, S, D)),
            "w1": _w((lambda *s: torch.randn(*s, dtype=torch.bfloat16) * 0.1)(D, F) * (2.0 / D) ** 0.5),
            "w3": _w((lambda *s: torch.randn(*s, dtype=torch.bfloat16) * 0.1)(D, F) * (2.0 / D) ** 0.5),
            "w2": _w((lambda *s: torch.randn(*s, dtype=torch.bfloat16) * 0.1)(F, D) * (2.0 / F) ** 0.5),
        }
    _B_in = _B_setup()

    return {
        "A_x": _A_in["x"],
        "A_k_cache": _A_in["k_cache"],
        "A_v_cache": _A_in["v_cache"],
        "A_wq": _A_in["wq"],
        "A_wo": _A_in["wo"],
        "B_x": _B_in["x"],
        "B_w1": _B_in["w1"],
        "B_w3": _B_in["w3"],
        "B_w2": _B_in["w2"],
        "gate_w": _w((lambda *s: torch.randn(*s, dtype=torch.bfloat16) * 0.05)(448) * 16.0),
    }

def reference(A_x, A_k_cache, A_v_cache, A_wq, A_wo, B_x, B_w1, B_w3, B_w2, gate_w):
    def _A_ref(x, k_cache, v_cache, wq, wo):
        B, Sq, D = x.shape
        Hq, Dh = 16, 256
        Hkv = k_cache.shape[1]
        rep = Hq // Hkv
        q = (lambda x, *d: x.permute(*d))((x @ wq).reshape(B, Sq, Hq, Dh), 0, 2, 1, 3)
        k = (lambda x, n, axis: torch.repeat_interleave(x, n, dim=axis))(k_cache, rep, axis=1)
        v = (lambda x, n, axis: torch.repeat_interleave(x, n, dim=axis))(v_cache, rep, axis=1)
        scores = torch.einsum("bhqd,bhkd->bhqk", q, k) * (Dh ** -0.5)
        p = _softmax(scores, axis=-1)
        o = torch.einsum("bhqk,bhkd->bhqd", p, v)
        return (lambda x, *d: x.permute(*d))(o, 0, 2, 1, 3).reshape(B, Sq, Hq * Dh) @ wo

    def _B_ref(x, w1, w3, w2):
        gate = torch.einsum("bsd,df->bsf", x, w1)
        up = torch.einsum("bsd,df->bsf", x, w3)
        h = _silu(gate) * up
        out = torch.einsum("bsf,fd->bsd", h, w2)
        return out

    x = A_x
    g = _sigmoid((x * gate_w).sum(-1, keepdims=True))
    a = _A_ref(x, A_k_cache, A_v_cache, A_wq, A_wo)
    b = _B_ref(x, B_w1, B_w3, B_w2)
    return g * a + (1 - g) * b
```

```cpp
#include <hip/hip_runtime.h>
#include <hip/hip_bf16.h>
#include <stdint.h>

typedef unsigned u32x4 __attribute__((ext_vector_type(4)));
typedef float f32x4 __attribute__((ext_vector_type(4)));
typedef __bf16 bf16x2 __attribute__((ext_vector_type(2)));
typedef unsigned short u16;

constexpr int B = 2, D = 448, HQ = 16, HKV = 8, SK = 32768, DH = 256, F = 1792;
constexpr int NSPLIT = 16, KEYS_PER_WG = SK / NSPLIT;
constexpr int WAVES = 8;

__device__ __forceinline__ float bf2f(u16 v) { return __uint_as_float((unsigned)v << 16); }
__device__ __forceinline__ float bflo(unsigned v) { return __uint_as_float(v << 16); }
__device__ __forceinline__ float bfhi(unsigned v) { return __uint_as_float(v & 0xffff0000u); }
__device__ __forceinline__ u16 f2bf(float f) { __hip_bfloat16 h = __float2bfloat16(f); return __builtin_bit_cast(u16, h); }
__device__ __forceinline__ float rbf(float f) { return bf2f(f2bf(f)); }
__device__ __forceinline__ float dot2(unsigned k, unsigned q, float acc) {
  return __builtin_amdgcn_fdot2_f32_bf16(__builtin_bit_cast(bf16x2, k), __builtin_bit_cast(bf16x2, q), acc, false);
}
template <int CTRL> __device__ __forceinline__ float dpp(float x) {
  return __builtin_bit_cast(float, __builtin_amdgcn_mov_dpp(__builtin_bit_cast(int, x), CTRL, 0xf, 0xf, true));
}
constexpr int XOR1 = 0xB1, XOR2 = 0x4E, XOR7 = 0x141, XOR8 = 0x128, ROR4 = 0x124;

__global__ void __launch_bounds__(256) k_prep(const u16* __restrict__ x, const u16* __restrict__ wq, const u16* __restrict__ w1,
                                              const u16* __restrict__ w3, u16* __restrict__ q, float* __restrict__ h) {
  __shared__ float xs[2][D];
  __shared__ float red[2][2][32][64 + 1];
  const int tid = threadIdx.x;
  for (int i = tid; i < 2 * D; i += 256) xs[i / D][i % D] = bf2f(x[i]);
  __syncthreads();
  const int c8 = tid & 7, r = tid >> 3;
  const bool isq = blockIdx.x < 64;
  const int col0 = isq ? blockIdx.x * 64 : (blockIdx.x - 64) * 64;
  const int ld = isq ? HQ * DH : F;
  const u16* wa = isq ? wq : w1;
  float a0[8], a1[8], b0[8], b1[8];
  for (int k = 0; k < 8; ++k) a0[k] = a1[k] = b0[k] = b1[k] = 0.f;
  for (int p = 0; p < 14; ++p) {
    const int row = p * 32 + r;
    const u32x4 w = *(const u32x4*)(wa + (size_t)row * ld + col0 + c8 * 8);
    const float x0 = xs[0][row], x1 = xs[1][row];
    for (int k = 0; k < 4; ++k) {
      a0[2 * k] += x0 * bflo(w[k]); a0[2 * k + 1] += x0 * bfhi(w[k]);
      a1[2 * k] += x1 * bflo(w[k]); a1[2 * k + 1] += x1 * bfhi(w[k]);
    }
    if (!isq) {
      const u32x4 u = *(const u32x4*)(w3 + (size_t)row * ld + col0 + c8 * 8);
      for (int k = 0; k < 4; ++k) {
        b0[2 * k] += x0 * bflo(u[k]); b0[2 * k + 1] += x0 * bfhi(u[k]);
        b1[2 * k] += x1 * bflo(u[k]); b1[2 * k + 1] += x1 * bfhi(u[k]);
      }
    }
  }
  for (int k = 0; k < 8; ++k) {
    red[0][0][r][c8 * 8 + k] = a0[k]; red[0][1][r][c8 * 8 + k] = a1[k];
    red[1][0][r][c8 * 8 + k] = b0[k]; red[1][1][r][c8 * 8 + k] = b1[k];
  }
  __syncthreads();
  if (tid < 128) {
    const int b = tid >> 6, c = tid & 63;
    float s = 0.f, t = 0.f;
    for (int i = 0; i < 32; ++i) { s += red[0][b][i][c]; t += red[1][b][i][c]; }
    if (isq) q[b * (HQ * DH) + col0 + c] = f2bf(s);
    else {
      const float g = rbf(s), u = rbf(t);
      const float si = rbf(g / (1.f + __expf(-g)));
      h[b * F + col0 + c] = rbf(si * u);
    }
  }
}

__global__ void __launch_bounds__(512, 2) k_attn(const u16* __restrict__ kc, const u16* __restrict__ vc, const u16* __restrict__ q,
                                                 float* __restrict__ pml, float* __restrict__ po) {
  __shared__ __attribute__((aligned(16))) float lds[WAVES * 64 + WAVES * 2 * (4 + DH)];
  const int wg = blockIdx.x;
  const int grp = wg / NSPLIT, s = wg % NSPLIT;
  const int b = grp >> 3, kvh = grp & 7;
  const int tid = threadIdx.x, lane = tid & 63, wave = __builtin_amdgcn_readfirstlane(tid >> 6);
  const int half = lane >> 5, c = lane & 31;
  constexpr float C2 = 0.0625f * 1.4426950408889634f;
  u32x4 qp[2];
  qp[0] = *(const u32x4*)(q + b * (HQ * DH) + (kvh * 2 + 0) * DH + c * 8);
  qp[1] = *(const u32x4*)(q + b * (HQ * DH) + (kvh * 2 + 1) * DH + c * 8);
  const size_t base = ((size_t)grp * SK + (size_t)s * KEYS_PER_WG) * DH;
  const u32x4* kp = (const u32x4*)(kc + base) + lane;
  const u32x4* vp = (const u32x4*)(vc + base) + lane;
  constexpr int NT = KEYS_PER_WG / 16 / WAVES;
  u32x4 Kn[8], Vn[8];
#define ISSUE(t_) do { const size_t o_ = (size_t)((t_) * WAVES + wave) * 512; \
    _Pragma("unroll") for (int i_ = 0; i_ < 8; ++i_) Kn[i_] = *(kp + o_ + i_ * 64); \
    _Pragma("unroll") for (int i_ = 0; i_ < 8; ++i_) Vn[i_] = __builtin_nontemporal_load(vp + o_ + i_ * 64); } while (0)
  float m = -INFINITY, l = 0.f, acc[16];
#pragma unroll
  for (int k = 0; k < 16; ++k) acc[k] = 0.f;
  ISSUE(0);
  float* prow = lds + wave * 64;
  for (int t = 0; t < NT; ++t) {
    u32x4 Kc[8], Vc[8];
#pragma unroll
    for (int i = 0; i < 8; ++i) { Kc[i] = Kn[i]; Vc[i] = Vn[i]; }
    if (t + 1 < NT) ISSUE(t + 1);
    float x[16];
#pragma unroll
    for (int i = 0; i < 8; ++i)
#pragma unroll
      for (int h = 0; h < 2; ++h) {
        float d = 0.f;
#pragma unroll
        for (int k = 0; k < 4; ++k) d = dot2(Kc[i][k], qp[h][k], d);
        x[i * 2 + h] = d;
      }
    const int c16 = lane & 15;
#define TR_STEP(HALF, CTRL, BIT) _Pragma("unroll") for (int hh = 0; hh < (HALF); ++hh) { \
      const float lo_ = x[hh], hi_ = x[hh + (HALF)]; const float t1_ = lo_ + dpp<CTRL>(lo_), t2_ = hi_ + dpp<CTRL>(hi_); \
      x[hh] = (c16 & (BIT)) ? t2_ : t1_; }
    TR_STEP(8, XOR8, 8) TR_STEP(4, XOR7, 4) TR_STEP(2, XOR2, 2) TR_STEP(1, XOR1, 1)
#undef TR_STEP
    float sc;
    {
      auto sw = __builtin_amdgcn_permlane16_swap(__float_as_uint(x[0]), __float_as_uint(x[0]), false, false);
      sc = __uint_as_float(sw[0]) + __uint_as_float(sw[1]);
    }
    sc *= C2;
    float pm = sc;
    pm = fmaxf(pm, dpp<XOR2>(pm)); pm = fmaxf(pm, dpp<ROR4>(pm)); pm = fmaxf(pm, dpp<XOR8>(pm));
    {
      auto sw = __builtin_amdgcn_permlane32_swap(__float_as_uint(pm), __float_as_uint(pm), false, false);
      pm = fmaxf(__uint_as_float(sw[0]), __uint_as_float(sw[1]));
    }
    const float mn = fmaxf(m, pm);
    const float alpha = __builtin_amdgcn_exp2f(m - mn);
    const float p = __builtin_amdgcn_exp2f(sc - mn);
    l = l * alpha + p; m = mn;
    const float al0 = dpp<0xA0>(alpha);
    const float al1 = dpp<0xF5>(alpha);
#pragma unroll
    for (int k = 0; k < 8; ++k) { acc[k] *= al0; acc[8 + k] *= al1; }
    prow[lane] = p;
    asm volatile("s_waitcnt lgkmcnt(0)" ::: "memory");
    f32x4 pr[4];
#pragma unroll
    for (int u = 0; u < 4; ++u) pr[u] = *(const f32x4*)(prow + half * 32 + 4 * u);
#pragma unroll
    for (int i = 0; i < 8; ++i) {
      const float p0 = pr[i >> 1][(i & 1) * 2], p1 = pr[i >> 1][(i & 1) * 2 + 1];
#pragma unroll
      for (int k = 0; k < 4; ++k) {
        const float vl = bflo(Vc[i][k]), vh = bfhi(Vc[i][k]);
        acc[2 * k] = fmaf(p0, vl, acc[2 * k]); acc[2 * k + 1] = fmaf(p0, vh, acc[2 * k + 1]);
        acc[8 + 2 * k] = fmaf(p1, vl, acc[8 + 2 * k]); acc[8 + 2 * k + 1] = fmaf(p1, vh, acc[8 + 2 * k + 1]);
      }
    }
  }
#undef ISSUE
#pragma unroll
  for (int k = 0; k < 16; ++k) {
    auto sw = __builtin_amdgcn_permlane32_swap(__float_as_uint(acc[k]), __float_as_uint(acc[k]), false, false);
    acc[k] = __uint_as_float(sw[0]) + __uint_as_float(sw[1]);
  }
  { float t = l; t += dpp<XOR2>(t); t += dpp<ROR4>(t); t += dpp<XOR8>(t);
    auto sw = __builtin_amdgcn_permlane32_swap(__float_as_uint(t), __float_as_uint(t), false, false);
    l = __uint_as_float(sw[0]) + __uint_as_float(sw[1]); }
  float* mrg = lds + WAVES * 64;
  if (lane < 32) {
#pragma unroll
    for (int h = 0; h < 2; ++h) {
      float* rec = mrg + (wave * 2 + h) * (4 + DH);
      *(f32x4*)(rec + 4 + 8 * c) = f32x4{acc[h * 8], acc[h * 8 + 1], acc[h * 8 + 2], acc[h * 8 + 3]};
      *(f32x4*)(rec + 4 + 8 * c + 4) = f32x4{acc[h * 8 + 4], acc[h * 8 + 5], acc[h * 8 + 6], acc[h * 8 + 7]};
      if (lane == h) { rec[0] = m; rec[1] = l; }
    }
  }
  __syncthreads();
  {
    const int h = tid >> 8, d = tid & 255;
    float M = -INFINITY;
#pragma unroll
    for (int w = 0; w < WAVES; ++w) M = fmaxf(M, mrg[(w * 2 + h) * (4 + DH)]);
    float L = 0.f, o = 0.f;
#pragma unroll
    for (int w = 0; w < WAVES; ++w) {
      const float* rec = mrg + (w * 2 + h) * (4 + DH);
      const float wt = __builtin_amdgcn_exp2f(rec[0] - M);
      L = fmaf(wt, rec[1], L); o = fmaf(wt, rec[4 + d], o);
    }
    const int row = (grp * 2 + h) * NSPLIT + s;
    po[(size_t)row * DH + d] = o;
    if (d == 0) { pml[row * 2] = M; pml[row * 2 + 1] = L; }
  }
}

__global__ void __launch_bounds__(256) k_comb(const float* __restrict__ pml, const float* __restrict__ po, float* __restrict__ o) {
  const int row = blockIdx.x, d = threadIdx.x;
  float M = -INFINITY;
  for (int s = 0; s < NSPLIT; ++s) M = fmaxf(M, pml[(row * NSPLIT + s) * 2]);
  float L = 0.f, a = 0.f;
  for (int s = 0; s < NSPLIT; ++s) {
    const float wt = __builtin_amdgcn_exp2f(pml[(row * NSPLIT + s) * 2] - M);
    L = fmaf(wt, pml[(row * NSPLIT + s) * 2 + 1], L);
    a = fmaf(wt, po[(size_t)(row * NSPLIT + s) * DH + d], a);
  }
  o[row * DH + d] = rbf(a / L);
}

__global__ void __launch_bounds__(1024) k_out(const u16* __restrict__ x, const u16* __restrict__ gw, const float* __restrict__ o, const float* __restrict__ h,
                                              const u16* __restrict__ wo, const u16* __restrict__ w2, u16* __restrict__ out) {
  __shared__ float red[2][2][512][17];
  __shared__ float gs[2];
  const int tid = threadIdx.x, c2 = tid & 1, r = tid >> 1, col0 = blockIdx.x * 16;
  float a0[8], a1[8], b0[8], b1[8];
  for (int k = 0; k < 8; ++k) a0[k] = a1[k] = b0[k] = b1[k] = 0.f;
  for (int row = r; row < HQ * DH; row += 512) {
    const u32x4 w = *(const u32x4*)(wo + (size_t)row * D + col0 + c2 * 8);
    const float x0 = o[row], x1 = o[HQ * DH + row];
    for (int k = 0; k < 4; ++k) {
      a0[2 * k] += x0 * bflo(w[k]); a0[2 * k + 1] += x0 * bfhi(w[k]);
      a1[2 * k] += x1 * bflo(w[k]); a1[2 * k + 1] += x1 * bfhi(w[k]);
    }
  }
  for (int row = r; row < F; row += 512) {
    const u32x4 w = *(const u32x4*)(w2 + (size_t)row * D + col0 + c2 * 8);
    const float x0 = h[row], x1 = h[F + row];
    for (int k = 0; k < 4; ++k) {
      b0[2 * k] += x0 * bflo(w[k]); b0[2 * k + 1] += x0 * bfhi(w[k]);
      b1[2 * k] += x1 * bflo(w[k]); b1[2 * k + 1] += x1 * bfhi(w[k]);
    }
  }
  for (int k = 0; k < 8; ++k) {
    red[0][0][r][c2 * 8 + k] = a0[k]; red[0][1][r][c2 * 8 + k] = a1[k];
    red[1][0][r][c2 * 8 + k] = b0[k]; red[1][1][r][c2 * 8 + k] = b1[k];
  }
  if (tid < 128) {
    const int b = tid >> 6, ln = tid & 63;
    float s = 0.f;
    for (int i = ln; i < D; i += 64) s += rbf(bf2f(x[b * D + i]) * bf2f(gw[i]));
    for (int mm = 32; mm >= 1; mm >>= 1) s += __shfl_xor(s, mm);
    if (ln == 0) { s = rbf(s); gs[b] = rbf(1.f / (1.f + __expf(-s))); }
  }
  __syncthreads();
  {
    const int oi = tid >> 4, pl = tid & 15;
    const int mat = oi >> 5, b = (oi >> 4) & 1, c = oi & 15;
    float s = 0.f;
    for (int i = pl; i < 512; i += 16) s += red[mat][b][i][c];
    for (int mm = 8; mm >= 1; mm >>= 1) s += __shfl_xor(s, mm);
    __syncthreads();
    if (pl == 0) red[mat][b][0][c] = s;
  }
  __syncthreads();
  if (tid < 32) {
    const int b = tid >> 4, c = tid & 15;
    const float a = rbf(red[0][b][0][c]), bb = rbf(red[1][b][0][c]), g = gs[b];
    out[b * D + col0 + c] = f2bf(rbf(g * a) + rbf(rbf(1.f - g) * bb));
  }
}

extern "C" void kernel_launch(void* const* d_in, const int* in_sizes, int n_in, void* d_out, int out_size, void* d_ws, size_t ws_size,
                              hipStream_t stream) {
  const u16* x = (const u16*)d_in[0];
  const u16* kc = (const u16*)d_in[1];
  const u16* vc = (const u16*)d_in[2];
  const u16* wq = (const u16*)d_in[3];
  const u16* wo = (const u16*)d_in[4];
  const u16* w1 = (const u16*)d_in[6];
  const u16* w3 = (const u16*)d_in[7];
  const u16* w2 = (const u16*)d_in[8];
  const u16* gw = (const u16*)d_in[9];
  char* ws = (char*)d_ws;
  u16* q = (u16*)ws;
  float* h = (float*)(ws + 16384);
  float* o = (float*)(ws + 32768);
  float* pml = (float*)(ws + 65536);
  float* po = (float*)(ws + 131072);
  k_prep<<<64 + 28, 256, 0, stream>>>(x, wq, w1, w3, q, h);
  k_attn<<<256, 512, 0, stream>>>(kc, vc, q, pml, po);
  k_comb<<<32, 256, 0, stream>>>(pml, po, o);
  k_out<<<28, 1024, 0, stream>>>(x, gw, o, h, wo, w2, (u16*)d_out);
}
```

```cpp
#include <hip/hip_runtime.h>
#include <stdint.h>

#define GAS __attribute__((address_space(1)))
#define LAS __attribute__((address_space(3)))
typedef unsigned short u16;
typedef unsigned u32x2 __attribute__((ext_vector_type(2)));
typedef unsigned u32x4 __attribute__((ext_vector_type(4)));
typedef float f32x2 __attribute__((ext_vector_type(2)));
typedef float f32x4 __attribute__((ext_vector_type(4)));
typedef short s16x4 __attribute__((ext_vector_type(4)));
typedef __bf16 bf16x8 __attribute__((ext_vector_type(8)));
typedef __bf16 bf16x2 __attribute__((ext_vector_type(2)));

namespace {
constexpr int D = 448, HQ = 16, SK = 32768, DH = 256, F = 1792;
constexpr int NWG = 256, NSPLIT = 16, KEYS_WG = SK / NSPLIT;
constexpr int NSW = 4;
constexpr int NTHR = (NSW + 1) * 64;
constexpr int NTILE = KEYS_WG / 16 / NSW;
constexpr int TSTR = NSW * 8192;
constexpr float C2 = 0.0625f * 1.4426950408889634f;
constexpr int DEPTH_0 = 6;
constexpr int DEPTH_K = 5, DEPTH_V = 6, DEPTH_E = 12;
constexpr int WIN_WQ = 12, WIN_WO = 12;
constexpr int AUX_SC1 = 16, AUX_NT = 2;
constexpr int WS_EPOCH = 0;
constexpr int WS_QG = 4096;
constexpr int WS_ML = WS_QG + 16 * 2048;
constexpr int WS_P1 = WS_ML + NWG * 128;
constexpr int WS_P2 = WS_P1 + NWG * 2048;
constexpr int WS_END = WS_P2 + NWG * D * 8;
constexpr unsigned SENT = 0xFFFFFFFFu;
constexpr int L_K = 0;
constexpr int L_S = L_K + NSW * 3 * 8192;
constexpr int L_W = L_S + NSW * 4096;
constexpr int L_X = L_W + 32 * D * 2;
constexpr int L_XB = L_X + D * 4;
constexpr int L_MP = L_XB + D * 2;
constexpr int L_Q = L_MP + 128;
constexpr int L_MLP = L_Q + 1024;
constexpr int L_G = L_MLP + D * 4;
constexpr int L_WML = L_G + 16;
constexpr int LDS_BYTES = L_WML + 64;
static_assert(LDS_BYTES <= 160 * 1024 && LDS_BYTES > 80 * 1024 && L_X % 16 == 0 && L_MP % 16 == 0 && L_MLP % 16 == 0, "lds map");
constexpr unsigned SPIN_CAP = 1u << 18;

__device__ __forceinline__ float bflo(unsigned v) { return __uint_as_float(v << 16); }
__device__ __forceinline__ float bfhi(unsigned v) { return __uint_as_float(v & 0xffff0000u); }
__device__ __forceinline__ unsigned pk_bf16(float lo, float hi) {
  bf16x2 r = {(__bf16)lo, (__bf16)hi};
  return __builtin_bit_cast(unsigned, r);
}
__device__ __forceinline__ float rbf(float f) { return bflo(pk_bf16(f, 0.f)); }
template <int CTRL> __device__ __forceinline__ float dpp(float x) {
  return __builtin_bit_cast(float, __builtin_amdgcn_mov_dpp(__builtin_bit_cast(int, x), CTRL, 0xf, 0xf, true));
}
template <int CTRL> __device__ __forceinline__ unsigned dppu(unsigned x) { return (unsigned)__builtin_amdgcn_mov_dpp((int)x, CTRL, 0xf, 0xf, true); }
constexpr int ROR1 = 0x121, ROR2 = 0x122, ROR4 = 0x124, ROR8 = 0x128, QP_XOR1 = 0xB1;
__device__ __forceinline__ float tr32(float a, float b) {
  auto s = __builtin_amdgcn_permlane32_swap(__float_as_uint(a), __float_as_uint(b), false, false);
  return __uint_as_float(s[0]) + __uint_as_float(s[1]);
}
__device__ __forceinline__ float tr16(float a, float b) {
  auto s = __builtin_amdgcn_permlane16_swap(__float_as_uint(a), __float_as_uint(b), false, false);
  return __uint_as_float(s[0]) + __uint_as_float(s[1]);
}
__device__ __forceinline__ float max_rows(float x) {
  auto s = __builtin_amdgcn_permlane16_swap(__float_as_uint(x), __float_as_uint(x), false, false);
  x = fmaxf(__uint_as_float(s[0]), __uint_as_float(s[1]));
  auto t = __builtin_amdgcn_permlane32_swap(__float_as_uint(x), __float_as_uint(x), false, false);
  return fmaxf(__uint_as_float(t[0]), __uint_as_float(t[1]));
}
__device__ __forceinline__ s16x4 lds_tr(LAS unsigned char* p) { return __builtin_amdgcn_ds_read_tr16_b64_v4i16((LAS s16x4*)p); }
__device__ __forceinline__ unsigned umax(unsigned a, unsigned b) { return a > b ? a : b; }
__device__ __forceinline__ unsigned umax4(u32x4 v) { return umax(umax(v.x, v.y), umax(v.z, v.w)); }
__device__ __forceinline__ unsigned tagdiff(u32x4 v, unsigned tag) { return (v.y ^ tag) | (v.w ^ tag); }
__device__ __forceinline__ s16x4 col_lo(unsigned r0, unsigned r1, unsigned r2, unsigned r3) {
  return __builtin_bit_cast(s16x4, u32x2{__builtin_amdgcn_perm(r1, r0, 0x05040100u), __builtin_amdgcn_perm(r3, r2, 0x05040100u)});
}
__device__ __forceinline__ s16x4 col_hi(unsigned r0, unsigned r1, unsigned r2, unsigned r3) {
  return __builtin_bit_cast(s16x4, u32x2{__builtin_amdgcn_perm(r1, r0, 0x07060302u), __builtin_amdgcn_perm(r3, r2, 0x07060302u)});
}
#define ALL_ARGS() asm volatile("" :: "s"(x_), "s"(kc_), "s"(vc_), "s"(wq_), "s"(wo_), "s"(w1_), "s"(w3_), "s"(w2_), "s"(gw_), "s"(out_), "s"(ws_))
#define SB() __builtin_amdgcn_sched_barrier(0)
#define CFENCE() asm volatile("" :: "s"(ws_) : "memory")
#define VMWAIT(n) do { SB(); asm volatile("s_waitcnt vmcnt(%0)" :: "i"(n) : "memory"); SB(); } while (0)

struct VT { u32x4 v[8]; };
struct Addr { const unsigned char* k; __amdgpu_buffer_rsrc_t rk, rv; unsigned ko, vo; int t0; };

template <int J, int W> __device__ __forceinline__ void dma_k(const unsigned char* kt, unsigned ko, unsigned img) {
  SB();
  asm volatile("s_mov_b32 m0, %2\n\ts_waitcnt vmcnt(%4)\n\tglobal_load_lds_dwordx4 %0, %1 offset:%3 nt"
               :: "v"(ko ^ (unsigned)(32 * J)), "s"(kt + 4096), "s"(img + 4096), "i"(1024 * J - 4096), "i"(W) : "memory");
  SB();
}
template <int J> __device__ __forceinline__ u32x4 ld_k(__amdgpu_buffer_rsrc_t rk, int to, unsigned ko) {
  return __builtin_amdgcn_raw_buffer_load_b128(rk, (int)((ko ^ (unsigned)(32 * J)) + 1024 * (J & 4)) + 1024 * (J & 3), to & (NTILE * TSTR - 1), AUX_NT);
}
template <int I> __device__ __forceinline__ u32x4 ld_v(__amdgpu_buffer_rsrc_t rv, int to, unsigned vo) {
  return __builtin_amdgcn_raw_buffer_load_b128(rv, (int)vo + ((I >> 1) * 512 + (I & 1) * 256), to & (NTILE * TSTR - 1), AUX_NT);
}

template <int IDX>
__device__ __forceinline__ void prologue(VT& va, VT& vb, VT& vc, const Addr& a, unsigned img0) {
  if constexpr (IDX < 48) {
    constexpr int t = IDX >> 3, sl = IDX & 7;
    if constexpr (t < 3) dma_k<sl, DEPTH_0 - 1>(a.k + a.t0 + t * TSTR, a.ko, img0 + t * 8192);
    else { VMWAIT(DEPTH_0 - 1); (t == 3 ? va : t == 4 ? vb : vc).v[sl] = ld_k<sl>(a.rk, a.t0 + t * TSTR, a.ko); }
    SB();
    prologue<IDX + 1>(va, vb, vc, a, img0);
  }
}

template <bool MOVE, int NEXT, int KWAIT>
__device__ __forceinline__ void k_step(const u32x4 (&qf)[8], float& m, unsigned img, unsigned kr, unsigned sw, bool wr,
                                       unsigned fre, VT& r, int kn, int vn, const Addr& a) {
  if constexpr (KWAIT > 0) VMWAIT(KWAIT);
  u32x4 kf[8];
#pragma unroll
  for (int i = 0; i < 8; ++i) kf[i] = *(const LAS u32x4*)(size_t)(img + (kr ^ (unsigned)((i & 3) * 64)) + (i >> 2) * 256);
  f32x4 s = {0.f, 0.f, 0.f, 0.f};
#define QK(i) { if constexpr (MOVE) *(LAS u32x4*)(size_t)(fre + (i) * 1024) = r.v[i]; \
    if constexpr (NEXT == 1) { VMWAIT(DEPTH_K - 1); r.v[i] = ld_k<i>(a.rk, kn, a.ko); SB(); } \
    if constexpr (NEXT == 2) { VMWAIT(DEPTH_V - 1); r.v[i] = ld_v<i>(a.rv, vn, a.vo); SB(); } \
    s = __builtin_amdgcn_mfma_f32_16x16x32_bf16(__builtin_bit_cast(bf16x8, kf[i]), __builtin_bit_cast(bf16x8, qf[i]), s, 0, 0, 0); }
  QK(0) QK(1) QK(2) QK(3) QK(4) QK(5) QK(6) QK(7)
#undef QK
  m = fmaxf(fmaxf(fmaxf(s[0], s[1]), fmaxf(s[2], s[3])), m);
  if (wr) *(LAS f32x4*)(size_t)sw = s;
}

template <bool RV, int DV = DEPTH_V>
__device__ __forceinline__ void v_step(const VT& v, VT& vf, f32x4 s, float nb, float& l, f32x4 (&acc)[2], int j8,
                                       int vn, const Addr& a) {
  const float p0 = __builtin_amdgcn_exp2f(fmaf(s[0], C2, nb)), p1 = __builtin_amdgcn_exp2f(fmaf(s[1], C2, nb));
  const float p2 = __builtin_amdgcn_exp2f(fmaf(s[2], C2, nb)), p3 = __builtin_amdgcn_exp2f(fmaf(s[3], C2, nb));
  l += (p0 + p1) + (p2 + p3);
  const unsigned pa = pk_bf16(p0, p1), pb = pk_bf16(p2, p3);
  s16x4 pj[8];
#pragma unroll
  for (int j = 0; j < 8; ++j) pj[j] = __builtin_bit_cast(s16x4, u32x2{j8 == j ? pa : 0u, j8 == j ? pb : 0u});
#define PV(h, w) { if constexpr (RV) { VMWAIT(DV - 1); vf.v[(h) * 4 + (w)] = ld_v<(h) * 4 + (w)>(a.rv, vn, a.vo); SB(); } \
    const unsigned r0 = v.v[h][w], r1 = v.v[2 + (h)][w], r2 = v.v[4 + (h)][w], r3 = v.v[6 + (h)][w]; \
    acc[h] = __builtin_amdgcn_mfma_f32_16x16x16bf16_1k(pj[2 * (w)], col_lo(r0, r1, r2, r3), acc[h], 0, 0, 0); \
    acc[h] = __builtin_amdgcn_mfma_f32_16x16x16bf16_1k(pj[2 * (w) + 1], col_hi(r0, r1, r2, r3), acc[h], 0, 0, 0); }
  PV(0, 0) PV(0, 1) PV(0, 2) PV(0, 3) PV(1, 0) PV(1, 1) PV(1, 2) PV(1, 3)
#undef PV
}

template <int N, int W, int I = 0>
__device__ __forceinline__ void paced(u32x4 (&r)[N], const GAS u32x4* p, int str) {
  if constexpr (I < N) {
    if constexpr (I >= W) VMWAIT(W - 1);
    r[I] = p[I * str];
    SB();
    paced<N, W, I + 1>(r, p, str);
  }
}

template <int I>
__device__ __forceinline__ void wq_loads(u32x4 (&r)[28], const GAS u32x4* p, int g0) {
  if constexpr (I < 28) {
    if constexpr (I >= WIN_WQ) VMWAIT(WIN_WQ - 1);
    const int G = g0 + (I >> 2) >= 7 ? g0 + (I >> 2) - 7 : g0 + (I >> 2);
    r[I] = (p + G * 64 * (HQ * DH / 8))[(I & 3) * (HQ * DH / 8)];
    SB();
    wq_loads<I + 1>(r, p, g0);
  }
}

__global__ void __launch_bounds__(NTHR, 2)
fused_kernel(const u16* __restrict__ x_, const u16* __restrict__ kc_, const u16* __restrict__ vc_, const u16* __restrict__ wq_,
             const u16* __restrict__ wo_, const u16* __restrict__ w1_, const u16* __restrict__ w3_, const u16* __restrict__ w2_,
             const u16* __restrict__ gw_, u16* __restrict__ out_, unsigned char* ws_) {
  extern __shared__ __attribute__((aligned(1024))) unsigned char smem[];
  LAS unsigned char* L = (LAS unsigned char*)smem;
  const int lane = threadIdx.x & 63, hw = __builtin_amdgcn_readfirstlane(threadIdx.x >> 6);
  const int wave = hw == 0 ? NSW : hw - 1, tid = (int)threadIdx.x - 64;
  const int wg = blockIdx.x, grp = wg >> 4, s = wg & 15, b = grp >> 3, kvh = grp & 7;
  const int n = lane & 15, g4 = lane >> 4;
  const int i16 = kvh * 2 + (s >> 3), lg = (i16 >> 2) * 8 + (s & 7);

  if (wave == NSW) {
    __builtin_amdgcn_s_setprio(3);
    ALL_ARGS();
    const __amdgpu_buffer_rsrc_t rs = __builtin_amdgcn_make_buffer_rsrc((void*)ws_, 0, WS_END, 0x00020000);
    const u32x2 ep = __builtin_amdgcn_raw_buffer_load_b64(rs, WS_EPOCH + b * 128, 0, AUX_SC1);
    {
      const u32x4 sent = {SENT, SENT, SENT, SENT};
      __builtin_amdgcn_raw_buffer_store_b128(sent, rs, WS_P1 + wg * 2048 + lane * 16, 0, AUX_SC1);
      __builtin_amdgcn_raw_buffer_store_b128(sent, rs, WS_P1 + wg * 2048 + 1024 + lane * 16, 0, AUX_SC1);
      if (lane < 8) __builtin_amdgcn_raw_buffer_store_b128(sent, rs, WS_ML + wg * 128 + lane * 16, 0, AUX_SC1);
      SB();
    }
    u32x4 xv = {0u, 0u, 0u, 0u}, gv = {0u, 0u, 0u, 0u};
    if (lane < 56) { xv = *((const GAS u32x4*)(x_ + b * D) + lane); gv = *((const GAS u32x4*)gw_ + lane); }
    const int jq = 2 * (s & 7) + (s >> 3);
    const GAS u32x4* wqp = (const GAS u32x4*)(wq_ + kvh * 512 + jq * 32) + (unsigned)((lane >> 2) * 4 * (HQ * DH / 8) + (lane & 3));
    u32x4 wv[28];
    const int g0 = ((b * 2 + (s >> 3)) * 7) >> 2;
    wq_loads<0>(wv, wqp, g0);
    if (lane < 56) *(LAS u32x4*)(L + L_XB + lane * 16) = xv;
    u32x2 xa[7];
#pragma unroll
    for (int G = 0; G < 7; ++G) xa[G] = *(const LAS u32x2*)(L + L_XB + (g0 + G >= 7 ? g0 + G - 7 : g0 + G) * 128 + (lane >> 2) * 8);
    const unsigned long long e64 = ((unsigned long long)ep.y << 32) | ep.x;
    const unsigned long long t64 = e64 * 6364136223846793005ull + 1442695040888963407ull;
    const unsigned tlo = __builtin_amdgcn_readfirstlane((unsigned)t64), thi = __builtin_amdgcn_readfirstlane((unsigned)(t64 >> 32));
    {
      f32x4 qa8[8];
#pragma unroll
      for (int k = 0; k < 8; ++k) qa8[k] = f32x4{0.f, 0.f, 0.f, 0.f};
#pragma unroll
      for (int G = 0; G < 7; ++G)
#pragma unroll
        for (int w = 0; w < 4; ++w) {
          const unsigned r0 = wv[4 * G][w], r1 = wv[4 * G + 1][w], r2 = wv[4 * G + 2][w], r3 = wv[4 * G + 3][w];
          qa8[2 * w] = __builtin_amdgcn_mfma_f32_4x4x4bf16_1k(__builtin_bit_cast(s16x4, xa[G]), col_lo(r0, r1, r2, r3), qa8[2 * w], 0, 0, 0);
          qa8[2 * w + 1] = __builtin_amdgcn_mfma_f32_4x4x4bf16_1k(__builtin_bit_cast(s16x4, xa[G]), col_hi(r0, r1, r2, r3), qa8[2 * w + 1], 0, 0, 0);
        }
      float a[4];
#pragma unroll
      for (int k = 0; k < 4; ++k) a[k] = tr32(qa8[k][0], qa8[k + 4][0]);
#pragma unroll
      for (int k = 0; k < 2; ++k) { a[k] = tr16(a[k], a[k + 2]); a[k] += dpp<ROR8>(a[k]); a[k] += dpp<ROR4>(a[k]); }
      const u32x2 gr = {pk_bf16(a[0], a[1]), tlo};
      asm volatile("s_waitcnt vmcnt(0)" ::: "memory");
      if ((lane & 12) == 0) __builtin_amdgcn_raw_buffer_store_b64(gr, rs, WS_QG + (grp * 256 + jq * 16 + (lane & 3) * 4 + g4) * 8, 0, AUX_SC1);
      SB();
    }
    float gs = 0.f;
#pragma unroll
    for (int k = 0; k < 4; ++k) gs += rbf(bflo(xv[k]) * bflo(gv[k])) + rbf(bfhi(xv[k]) * bfhi(gv[k]));
    gs += dpp<ROR8>(gs); gs += dpp<ROR4>(gs); gs += dpp<ROR2>(gs); gs += dpp<ROR1>(gs);
    gs = tr16(gs, gs); gs = tr32(gs, gs);
    gs = rbf(gs);
    const float gate = rbf(1.f / (1.f + __expf(-gs)));
    if (lane == 0) *(LAS u32x4*)(L + L_G) = u32x4{__float_as_uint(gate), tlo, thi, 0u};
    if (lane < 56) {
      *(LAS f32x4*)(L + L_X + lane * 32) = f32x4{bflo(xv[0]), bfhi(xv[0]), bflo(xv[1]), bfhi(xv[1])};
      *(LAS f32x4*)(L + L_X + lane * 32 + 16) = f32x4{bflo(xv[2]), bfhi(xv[2]), bflo(xv[3]), bfhi(xv[3])};
    }
    u32x4 qa, qb;
    __builtin_amdgcn_s_sleep(32);
    for (unsigned spin = 0; spin < SPIN_CAP; ++spin) {
      CFENCE();
      qa = __builtin_amdgcn_raw_buffer_load_b128(rs, WS_QG + grp * 2048 + lane * 16, 0, AUX_SC1);
      qb = __builtin_amdgcn_raw_buffer_load_b128(rs, WS_QG + grp * 2048 + 1024 + lane * 16, 0, AUX_SC1);
      if (__all(qa.y == tlo && qa.w == tlo && qb.y == tlo && qb.w == tlo)) break;
    }
    *(LAS u32x2*)(L + L_Q + lane * 8) = u32x2{qa.x, qa.z};
    *(LAS u32x2*)(L + L_Q + 512 + lane * 8) = u32x2{qb.x, qb.z};
    __syncthreads();
    {
      const bool mlp_on = lg < 28;
      const int fcol = lg * 64 + (i16 & 3) * 16;
      if (mlp_on) {
        const LAS float* xs = (const LAS float*)(L + L_X);
        const int blk0 = (lg + (((b * 4 + (i16 & 3)) * 7) >> 2)) % 14, blk1 = blk0 == 13 ? 0 : blk0 + 1;
        const unsigned wlo = (unsigned)((lane >> 1) * (F / 8) + (lane & 1));
        {
          const GAS u32x4* p1 = (const GAS u32x4*)(w1_ + fcol) + wlo;
          const GAS u32x4* p3 = (const GAS u32x4*)(w3_ + fcol) + wlo;
          const u32x4 t0 = p1[blk0 * 32 * (F / 8)], t1 = p1[blk1 * 32 * (F / 8)], t2 = p3[blk0 * 32 * (F / 8)], t3 = p3[blk1 * 32 * (F / 8)];
          u32x4 r[7];
          paced<7, 7>(r, (const GAS u32x4*)(w2_ + fcol * D) + (unsigned)(b * 7 * 64 + lane), 64);
          asm volatile("" :: "v"(t0), "v"(t1), "v"(t2), "v"(t3));
#pragma unroll
          for (int i = 0; i < 7; ++i) *(LAS u32x4*)(L + L_W + ((b * 7 + i) * 64 + lane) * 16) = r[i];
          __builtin_amdgcn_s_sleep(32);
        }
#pragma unroll
        for (int mat = 0; mat < 2; ++mat) {
          float a[8];
#pragma unroll
          for (int k = 0; k < 8; ++k) a[k] = 0.f;
          const GAS u32x4* wp = (const GAS u32x4*)((mat ? w3_ : w1_) + fcol) + wlo;
          int blk = blk0;
#pragma unroll 1
          for (int i = 0; i < 14; ++i) {
            const u32x4 w = wp[blk * 32 * (F / 8)];
            const float xr = xs[blk * 32 + (lane >> 1)];
#pragma unroll
            for (int k = 0; k < 4; ++k) { a[2 * k] = fmaf(xr, bflo(w[k]), a[2 * k]); a[2 * k + 1] = fmaf(xr, bfhi(w[k]), a[2 * k + 1]); }
            blk = blk == 13 ? 0 : blk + 1;
          }
#pragma unroll
          for (int k = 0; k < 4; ++k) a[k] = tr32(a[k], a[k + 4]);
#pragma unroll
          for (int k = 0; k < 2; ++k) { a[k] = tr16(a[k], a[k + 2]); a[k] += dpp<ROR8>(a[k]); a[k] += dpp<ROR4>(a[k]); a[k] += dpp<ROR2>(a[k]); }
          if ((lane & 14) == 0) *(LAS f32x2*)(L + L_MP + (mat * 16 + (lane & 1) * 8 + 2 * g4) * 4) = f32x2{a[0], a[1]};
        }
        {
          u32x4 r[7];
          paced<7, 7>(r, (const GAS u32x4*)(w2_ + fcol * D) + (unsigned)((b ^ 1) * 7 * 64 + lane), 64);
#pragma unroll
          for (int i = 0; i < 7; ++i) *(LAS u32x4*)(L + L_W + (((b ^ 1) * 7 + i) * 64 + lane) * 16) = r[i];
        }
        const LAS f32x4* mp = (const LAS f32x4*)(L + L_MP);
        const f32x4 gt = mp[g4], up = mp[4 + g4];
        float h[4];
#pragma unroll
        for (int k = 0; k < 4; ++k) {
          const float gg = rbf(gt[k]), uu = rbf(up[k]);
          h[k] = rbf(gg / (1.f + __expf(-gg))) * uu;
        }
        const s16x4 hb = __builtin_bit_cast(s16x4, u32x2{pk_bf16(h[0], h[1]), pk_bf16(h[2], h[3])});
        const float og = rbf(1.f - gate);
        LAS unsigned char* w2a = L + L_W + (4 * g4 + (n >> 2)) * (D * 2) + (n & 3) * 8;
#pragma unroll
        for (int t4 = 0; t4 < 2; ++t4) {
          f32x4 r = {0.f, 0.f, 0.f, 0.f};
#pragma unroll
          for (int t = 0; t < 14; ++t) {
            f32x4 d = {0.f, 0.f, 0.f, 0.f};
            d = __builtin_amdgcn_mfma_f32_16x16x16bf16_1k(lds_tr(w2a + (t4 * 14 + t) * 32), hb, d, 0, 0, 0);
            if (n == t) r = d;
          }
          if (n < 14) *(LAS f32x4*)(L + L_MLP + ((t4 * 14 + n) * 16 + 4 * g4) * 4) = r * og;
        }
      } else {
        *(LAS f32x4*)(L + L_MLP + lane * 16) = f32x4{0.f, 0.f, 0.f, 0.f};
        if (lane < 48) *(LAS f32x4*)(L + L_MLP + 1024 + lane * 16) = f32x4{0.f, 0.f, 0.f, 0.f};
      }
    }
    {
#pragma unroll 1
      for (unsigned spin = 0; spin < SPIN_CAP && __builtin_amdgcn_readfirstlane(*(volatile LAS unsigned*)(L + L_G + 12)) == 0u; ++spin) __builtin_amdgcn_s_sleep(4);
      const unsigned char* wp = (const unsigned char*)(wo_ + (kvh * 512 + s * 32) * D);
      const unsigned dst = (unsigned)(size_t)(L + L_W), lo = lane * 16;
#pragma unroll
      for (int i = 0; i < 28; ++i) {
        const int o = (i < 14 ? i + 14 * b : i - 14 * b) * 1024;
        asm volatile("s_mov_b32 m0, %2\n\ts_waitcnt vmcnt(%3)\n\tglobal_load_lds_dwordx4 %0, %1" :: "v"(lo), "s"(wp + o), "s"(dst + o), "i"(WIN_WO - 1) : "memory");
      }
      asm volatile("s_waitcnt vmcnt(0)" ::: "memory");
    }
    __syncthreads();
    return;
  }

  ALL_ARGS();
  const __amdgpu_buffer_rsrc_t rs = __builtin_amdgcn_make_buffer_rsrc((void*)ws_, 0, WS_END, 0x00020000);
  Addr ad;
  {
    const size_t kvbase = ((size_t)grp * SK + (size_t)s * KEYS_WG + wave * 16) * (DH * 2);
    ad.k = (const unsigned char*)kc_ + kvbase;
    ad.rk = __builtin_amdgcn_make_buffer_rsrc((void*)ad.k, 0, (NTILE - 1) * TSTR + 8192, 0x00020000);
    ad.rv = __builtin_amdgcn_make_buffer_rsrc((void*)((const unsigned char*)vc_ + kvbase), 0, (NTILE - 1) * TSTR + 8192, 0x00020000);
    ad.t0 = (s & 1) * 12 * TSTR;
    const unsigned hi = lane >> 5, p = lane & 31;
    ad.ko = hi * 512 + (p & 16) * 16 + (((p & 15) ^ hi) * 16);
    ad.vo = g4 * 2048 + n * 16;
  }
  const unsigned img0 = (unsigned)(size_t)(L + L_K) + wave * (3 * 8192);
  const unsigned kr = n * 512 + ((g4 ^ n) * 16);
  const unsigned sp0 = (unsigned)(size_t)(L + L_S) + wave * 4096 + g4 * 32 + (n & 1) * 16;
  VT va, vb, vc, vd;
  prologue<0>(va, vb, vc, ad, img0);
  __syncthreads();
  float m = -INFINITY;
  {
    u32x4 qf[8];
#pragma unroll
    for (int i = 0; i < 8; ++i) qf[i] = *(const LAS u32x4*)(L + L_Q + (n & 1) * 512 + i * 64 + g4 * 16);
    const bool wr = n < 2;
    const unsigned img1 = img0 + 8192, img2 = img0 + 16384, w0 = img0 + lane * 16, w1 = w0 + 8192, w2 = w0 + 16384;
    k_step<false, 0, 40>(qf, m, img0, kr, sp0, wr, w0, vd, 0, 0, ad);
    unsigned sw = sp0 + 128;
    int kn = ad.t0 + 6 * TSTR;
    for (int it = 0; it < 8; ++it) {
      k_step<true, 1, 32>(qf, m, img1, kr, sw, wr, w0, va, kn, 0, ad);
      k_step<true, 1, 32>(qf, m, img2, kr, sw + 128, wr, w1, vb, kn + TSTR, 0, ad);
      k_step<true, 1, 0>(qf, m, img0, kr, sw + 256, wr, w2, vc, kn + 2 * TSTR, 0, ad);
      sw += 384; kn += 3 * TSTR;
    }
    k_step<true, 1, 0>(qf, m, img1, kr, sw, wr, w0, va, kn, 0, ad);
    k_step<true, 1, 0>(qf, m, img2, kr, sw + 128, wr, w1, vb, kn + TSTR, 0, ad);
    k_step<true, 2, 0>(qf, m, img0, kr, sw + 256, wr, w2, vc, kn, ad.t0, ad);
    k_step<true, 2, 0>(qf, m, img1, kr, sw + 384, wr, w0, va, kn, ad.t0 + TSTR, ad);
    k_step<true, 2, 0>(qf, m, img2, kr, sw + 512, wr, w1, vb, kn, ad.t0 + 2 * TSTR, ad);
    k_step<false, 2, 0>(qf, m, img0, kr, sw + 640, wr, w2, vd, kn, ad.t0 + 3 * TSTR, ad);
    k_step<false, 0, 0>(qf, m, img1, kr, sw + 768, wr, w0, vd, kn, 0, ad);
  }
  m = max_rows(m);
  float l = 0.f;
  f32x4 acc[2] = {{0.f, 0.f, 0.f, 0.f}, {0.f, 0.f, 0.f, 0.f}};
  {
    const float nb = -m * C2;
    const int up = n >> 1;
    const LAS f32x4* sp = (const LAS f32x4*)(size_t)sp0;
    int vn = ad.t0 + 4 * TSTR;
    v_step<false>(vc, vd, sp[0], nb, l, acc, up, vn, ad);
    for (int it = 0; it < 6; ++it) {
      const f32x4 s1 = sp[8], s2 = sp[16], s3 = sp[24], s4 = sp[32];
      v_step<true>(va, vc, s1, nb, l, acc, up, vn, ad);
      v_step<true>(vb, va, s2, nb, l, acc, up, vn + TSTR, ad);
      v_step<true>(vd, vb, s3, nb, l, acc, up, vn + 2 * TSTR, ad);
      v_step<true>(vc, vd, s4, nb, l, acc, up, vn + 3 * TSTR, ad);
      sp += 32; vn += 4 * TSTR;
    }
    {
      const f32x4 s1 = sp[8], s2 = sp[16], s3 = sp[24], s4 = sp[32];
      v_step<true>(va, vc, s1, nb, l, acc, up, vn, ad);
      v_step<true>(vb, va, s2, nb, l, acc, up, vn + TSTR, ad);
      v_step<true, DEPTH_E>(vd, vb, s3, nb, l, acc, up, vn + 2 * TSTR, ad);
      if (tid == 0) *(volatile LAS unsigned*)(L + L_G + 12) = 1u;
      v_step<true, DEPTH_E>(vc, vd, s4, nb, l, acc, up, vn + 3 * TSTR, ad);
      sp += 32; vn += 4 * TSTR;
    }
    const f32x4 s1 = sp[8], s2 = sp[16], s3 = sp[24];
    v_step<false>(va, vc, s1, nb, l, acc, up, vn, ad);
    v_step<false>(vb, vc, s2, nb, l, acc, up, vn, ad);
    {
      const float q0 = __builtin_amdgcn_exp2f(fmaf(s3[0], C2, nb)), q1 = __builtin_amdgcn_exp2f(fmaf(s3[1], C2, nb));
      const float q2 = __builtin_amdgcn_exp2f(fmaf(s3[2], C2, nb)), q3 = __builtin_amdgcn_exp2f(fmaf(s3[3], C2, nb));
      float lw = l + ((q0 + q1) + (q2 + q3));
      lw = tr16(lw, lw); lw = tr32(lw, lw);
      if (lane < 2) *(LAS f32x2*)(L + L_WML + (wave * 2 + lane) * 8) = f32x2{m * C2, lw};
      SB();
    }
    v_step<false>(vd, vc, s3, nb, l, acc, up, vn, ad);
  }
  {
    LAS float* od = (LAS float*)(L + L_K + wave * (3 * 8192)) + n * 8 + g4 * 2;
    *(LAS f32x2*)od = f32x2{acc[0][0], acc[0][2]};
    *(LAS f32x2*)(od + 128) = f32x2{acc[1][0], acc[1][2]};
    *(LAS f32x2*)(od + 256) = f32x2{acc[0][1], acc[0][3]};
    *(LAS f32x2*)(od + 384) = f32x2{acc[1][1], acc[1][3]};
  }
  const int o_p1 = WS_P1 + wg * 2048 + tid * 16, o_ml = WS_ML + (grp * 16 + wave * 8 + lane) * 128 + s * 8;
  const LAS f32x2* wml = (const LAS f32x2*)(L + L_WML) + (wave & 1);
  const LAS f32x4* os = (const LAS f32x4*)(L + L_K) + (tid & 127);
  asm volatile("" :: "v"(o_p1), "v"(o_ml), "v"(os));
  __syncthreads();
  const f32x2 r0 = wml[0], r1 = wml[2], r2 = wml[4], r3 = wml[6];
  const f32x4 c0 = os[0], c1 = os[1536], c2 = os[3072], c3 = os[4608];
  const u32x4 gt = *(const LAS u32x4*)(L + L_G);
  const unsigned tag = gt.y;
  SB();
  if (wave < 2) {
    const float M = fmaxf(fmaxf(r0.x, r1.x), fmaxf(r2.x, r3.x));
    const float e0 = __builtin_amdgcn_exp2f(r0.x - M), e1 = __builtin_amdgcn_exp2f(r1.x - M);
    const float e2 = __builtin_amdgcn_exp2f(r2.x - M), e3 = __builtin_amdgcn_exp2f(r3.x - M);
    const f32x4 o = (c0 * e0 + c1 * e1) + (c2 * e2 + c3 * e3);
    __builtin_amdgcn_raw_buffer_store_b128(__builtin_bit_cast(u32x4, o), rs, o_p1, 0, AUX_SC1);
    const float Lw = (r0.y * e0 + r1.y * e1) + (r2.y * e2 + r3.y * e3);
    if (lane < 8) __builtin_amdgcn_raw_buffer_store_b64(u32x2{__float_as_uint(M), __float_as_uint(Lw)}, rs, o_ml, 0, AUX_SC1);
  }
  SB();
  u32x4 wof[7];
  LAS unsigned char* wa = L + L_W + (8 * g4 + (n >> 2)) * (D * 2) + wave * 224 + (n & 3) * 8;
#pragma unroll
  for (int t = 0; t < 7; ++t) {
    const u32x2 lo = __builtin_bit_cast(u32x2, lds_tr(wa + t * 32)), hi = __builtin_bit_cast(u32x2, lds_tr(wa + t * 32 + 4 * D * 2));
    wof[t] = u32x4{lo.x, lo.y, hi.x, hi.y};
  }
  const float gate = __uint_as_float(gt.x);
  const f32x2 mlp = *(const LAS f32x2*)(L + L_MLP + (wave * 112 + (n < 14 ? n >> 1 : 0) * 16 + 4 * g4 + 2 * (n & 1)) * 4);
  SB(); __builtin_amdgcn_s_barrier(); SB();
  const int o_p2 = WS_P2 + wg * (D * 8) + (wave * 112 + (n >> 1) * 16 + 4 * g4 + 2 * (n & 1)) * 8;
  u32x4 pw = {0u, 0u, 0u, 0u};
  if (n < 14) pw = __builtin_amdgcn_raw_buffer_load_b128(rs, o_p2, 0, AUX_SC1);
  {
    const int sp = (lane >> 1) & 7, cb = lane & 1;
    const int od = WS_P1 + (grp * 16 + sp) * 2048 + s * 128 + (2 * g4 + cb) * 16;
    const int om = WS_ML + wg * 128 + sp * 8;
    f32x2 r;
    u32x4 da, db; u32x2 ma, mb;
#pragma unroll 1
    for (unsigned spin = 0; spin < SPIN_CAP; ++spin) {
      CFENCE();
      da = __builtin_amdgcn_raw_buffer_load_b128(rs, od, 0, AUX_SC1);
      db = __builtin_amdgcn_raw_buffer_load_b128(rs, od + 8 * 2048, 0, AUX_SC1);
      ma = __builtin_amdgcn_raw_buffer_load_b64(rs, om, 0, AUX_SC1);
      mb = __builtin_amdgcn_raw_buffer_load_b64(rs, om + 64, 0, AUX_SC1);
      if (__builtin_amdgcn_ballot_w64(umax(umax(umax4(da), umax4(db)), umax(umax(ma.x, ma.y), umax(mb.x, mb.y))) == SENT) == 0ull) break;
    }
    asm volatile("" :: "v"(pw));
    {
      const float Ma = __uint_as_float(ma.x), Mb = __uint_as_float(mb.x);
      float M = fmaxf(Ma, Mb);
      asm volatile("s_nop 1\n\tv_max_f32_dpp %0, %0, %0 row_ror:8 row_mask:0xf bank_mask:0xf\n\ts_nop 1\n\tv_max_f32_dpp %0, %0, %0 row_ror:4 row_mask:0xf bank_mask:0xf\n\t"
          "s_nop 1\n\tv_max_f32_dpp %0, %0, %0 row_ror:2 row_mask:0xf bank_mask:0xf" : "+v"(M));
      const float ea = __builtin_amdgcn_exp2f(Ma - M), eb = __builtin_amdgcn_exp2f(Mb - M);
      float Ls = fmaf(__uint_as_float(ma.y), ea, __uint_as_float(mb.y) * eb);
      const f32x4 ov = __builtin_bit_cast(f32x4, da) * ea + __builtin_bit_cast(f32x4, db) * eb;
      float o[4] = {ov[0], ov[1], ov[2], ov[3]};
#define ADDROR(k) "v_add_f32_dpp %0, %0, %0 row_ror:" #k " row_mask:0xf bank_mask:0xf\n\tv_add_f32_dpp %1, %1, %1 row_ror:" #k " row_mask:0xf bank_mask:0xf\n\t" \
      "v_add_f32_dpp %2, %2, %2 row_ror:" #k " row_mask:0xf bank_mask:0xf\n\tv_add_f32_dpp %3, %3, %3 row_ror:" #k " row_mask:0xf bank_mask:0xf\n\t" \
      "v_add_f32_dpp %4, %4, %4 row_ror:" #k " row_mask:0xf bank_mask:0xf\n\t"
      asm volatile("s_nop 1\n\t" ADDROR(8) ADDROR(4) ADDROR(2) "s_nop 0" : "+v"(Ls), "+v"(o[0]), "+v"(o[1]), "+v"(o[2]), "+v"(o[3]));
#undef ADDROR
      const float inv = __builtin_amdgcn_rcpf(Ls);
      const unsigned o0 = pk_bf16(o[0] * inv, o[1] * inv), o1 = pk_bf16(o[2] * inv, o[3] * inv);
      const unsigned x0 = dppu<QP_XOR1>(o0), x1 = dppu<QP_XOR1>(o1);
      const u32x4 of = cb ? u32x4{x0, x1, o0, o1} : u32x4{o0, o1, x0, x1};
      f32x4 d = {0.f, 0.f, 0.f, 0.f};
#pragma unroll
      for (int t = 0; t < 7; ++t) {
        const u32x4 oft = (n >> 1) == t ? of : u32x4{0u, 0u, 0u, 0u};
        d = __builtin_amdgcn_mfma_f32_16x16x32_bf16(__builtin_bit_cast(bf16x8, wof[t]), __builtin_bit_cast(bf16x8, oft), d, 0, 0, 0);
      }
      r = (cb ? f32x2{d[2], d[3]} : f32x2{d[0], d[1]}) * gate + mlp;
    }
    if (n < 14) __builtin_amdgcn_raw_buffer_store_b128(u32x4{__float_as_uint(r.x), tag, __float_as_uint(r.y), tag}, rs, o_p2, 0, AUX_SC1);
  }
  SB(); __builtin_amdgcn_s_barrier(); SB();
  if (wave == 3 && lg < 28) {
    const int col = lg * 16 + (i16 & 3) * 4;
    const int off = WS_P2 + (b * 128 + (lane >> 1)) * (D * 8) + (col + 2 * (lane & 1)) * 8;
    u32x4 d0, d1, d2, d3;
#pragma unroll 1
    for (unsigned spin = 0; spin < SPIN_CAP; ++spin) {
      CFENCE();
      d0 = __builtin_amdgcn_raw_buffer_load_b128(rs, off, 0, AUX_SC1);
      d1 = __builtin_amdgcn_raw_buffer_load_b128(rs, off + 32 * (D * 8), 0, AUX_SC1);
      d2 = __builtin_amdgcn_raw_buffer_load_b128(rs, off + 64 * (D * 8), 0, AUX_SC1);
      d3 = __builtin_amdgcn_raw_buffer_load_b128(rs, off + 96 * (D * 8), 0, AUX_SC1);
      if (__builtin_amdgcn_ballot_w64(((tagdiff(d0, tag) | tagdiff(d1, tag)) | (tagdiff(d2, tag) | tagdiff(d3, tag))) != 0u) == 0ull) break;
    }
    const float v0 = (__uint_as_float(d0.x) + __uint_as_float(d1.x)) + (__uint_as_float(d2.x) + __uint_as_float(d3.x));
    const float v1 = (__uint_as_float(d0.z) + __uint_as_float(d1.z)) + (__uint_as_float(d2.z) + __uint_as_float(d3.z));
    float u = tr32(v0, v1);
    u = tr16(u, u);
    u += dpp<ROR8>(u); u += dpp<ROR4>(u); u += dpp<ROR2>(u);
    if ((lane & 30) == 0) out_[b * D + col + 2 * (lane & 1) + (lane >> 5)] = (u16)pk_bf16(u, 0.f);
    if (lg == 0 && i16 == 0 && lane == 0) __builtin_amdgcn_raw_buffer_store_b64(u32x2{gt.y, gt.z}, rs, WS_EPOCH + b * 128, 0, AUX_SC1);
  }
}
}

extern "C" void kernel_launch(void* const* d_in, const int* in_sizes, int n_in, void* d_out, int out_size, void* d_ws, size_t ws_size,
                              hipStream_t stream) {
  (void)in_sizes; (void)n_in; (void)out_size; (void)ws_size;
  (void)hipFuncSetAttribute((const void*)fused_kernel, hipFuncAttributeMaxDynamicSharedMemorySize, LDS_BYTES);
  fused_kernel<<<NWG, NTHR, LDS_BYTES, stream>>>((const u16*)d_in[0], (const u16*)d_in[1], (const u16*)d_in[2], (const u16*)d_in[3],
                                                  (const u16*)d_in[4], (const u16*)d_in[6], (const u16*)d_in[7], (const u16*)d_in[8],
                                                  (const u16*)d_in[9], (u16*)d_out, (unsigned char*)d_ws);
}
```
